```python
import torch

def _softmax(x, axis=-1):
    return torch.softmax(x, dim=axis)
def _gelu(x):
    return torch.nn.functional.gelu(x, approximate="tanh")
def _silu(x):
    return torch.nn.functional.silu(x)
def _sigmoid(x):
    return torch.sigmoid(x)
def _layernorm(x, axis=-1, eps=1e-5):
    m = x.mean(dim=axis, keepdim=True)
    v = x.var(dim=axis, keepdim=True, unbiased=False)
    return (x - m) / torch.sqrt(v + eps)
def _scatter_add(tgt, idx, src, axis=0):
    return tgt.clone().index_add_(axis, idx, src)
def _segment_sum(src, seg, n):
    out = torch.zeros((n,) + src.shape[1:], dtype=src.dtype)
    return out.index_add_(0, seg, src)
def _pad(x, pw):
    flat = tuple(v for pair in reversed(pw) for v in pair)
    return torch.nn.functional.pad(x, flat)

kernel_name = 'standalone__mha_block_cached__near_ridge'

def setup_inputs():

    _w = (lambda a: (lambda t: t.to(torch.bfloat16) if t.is_floating_point() else t)(torch.as_tensor(a)))
    B, Sq, D = 8, 96, 1024
    H, Sk, Dh = 32, 512, 128
    return {
        "x": _w((lambda *s: torch.randn(*s, dtype=torch.bfloat16) * 0.8)(B, Sq, D)),
        "k_cache": _w((lambda *s: torch.randn(*s, dtype=torch.bfloat16) * 0.8)(B, H, Sk, Dh)),
        "v_cache": _w((lambda *s: torch.randn(*s, dtype=torch.bfloat16) * 0.05)(B, H, Sk, Dh)),
        "wq": _w((lambda *s: torch.randn(*s, dtype=torch.bfloat16) * 0.8)(D, H * Dh) * (D ** -0.5)),
        "wo": _w((lambda *s: torch.randn(*s, dtype=torch.bfloat16) * 0.05)(H * Dh, D) * ((H * Dh) ** -0.5)),
    }

def reference(x, k_cache, v_cache, wq, wo):
    B, Sq, D = x.shape
    H, Dh = 32, 128
    q = (lambda x, *d: x.permute(*d))((x @ wq).reshape(B, Sq, H, Dh), 0, 2, 1, 3)
    scores = torch.einsum("bhqd,bhkd->bhqk", q, k_cache) * (Dh ** -0.5)
    p = _softmax(scores, axis=-1)
    o = torch.einsum("bhqk,bhkd->bhqd", p, v_cache)
    return (lambda x, *d: x.permute(*d))(o, 0, 2, 1, 3).reshape(B, Sq, H * Dh) @ wo
```

```cpp
#include <hip/hip_runtime.h>
#include <hip/hip_bf16.h>
#include <utility>

typedef unsigned u32;
typedef unsigned long long u64;
typedef u32 v4u __attribute__((ext_vector_type(4)));
typedef u32 v2u __attribute__((ext_vector_type(2)));
typedef float f32x4 __attribute__((ext_vector_type(4)));
typedef float f32x16 __attribute__((ext_vector_type(16)));
typedef float f32x2 __attribute__((ext_vector_type(2)));
typedef short s16x4 __attribute__((ext_vector_type(4)));
typedef __bf16 bf16x8 __attribute__((ext_vector_type(8)));
typedef __bf16 bf16x2 __attribute__((ext_vector_type(2)));
#define LAS __attribute__((address_space(3)))
#define GAS __attribute__((address_space(1)))
#define DI __device__ __forceinline__
#define SB() __builtin_amdgcn_sched_barrier(0)
#define RLX_AGENT __ATOMIC_RELAXED, __HIP_MEMORY_SCOPE_AGENT

constexpr int SPLIT = 3;
constexpr int LDS_BYTES = 163840;
constexpr int SLOT = 28672;
constexpr int P_OFF = 0;
constexpr int Q_OFF = 49152;
constexpr int ST_OFF = 73728;
constexpr int BAD_OFF = ST_OFF + 1536;
constexpr int SUM_OFF = ST_OFF + 2048;
constexpr int SCR_OFF = ST_OFF + 5120;
constexpr int V_OFF = 81920;
constexpr size_t WS_EPOCH = 0;
constexpr size_t WS_B = 2u << 20, WS_BSZ = 2u << 20;
constexpr u32 B_F = 0;
constexpr u32 B_X = 851968;
constexpr u32 B_O = 65536;
constexpr u32 B_P = 1u << 20;
constexpr u32 SPIN_CAP = 1u << 21;

template <int I> using IC = std::integral_constant<int, I>;
template <int B, class F, int... I> DI void sfor_impl(F&& f, std::integer_sequence<int, I...>) { (f(IC<B + I>{}), ...); }
template <int B, int E, class F> DI void sfor(F&& f) { if constexpr (E > B) sfor_impl<B>(f, std::make_integer_sequence<int, E - B>{}); }

DI v4u make_srd(const void* p, u32 bytes) {
  u64 a = (u64)p;
  v4u r = {(u32)a, (u32)(a >> 32) & 0xffffu, bytes, 0x20000u};
  return r;
}
template <int POL, int MI, int SI> DI void dma16(u32 mb, u32 voff, v4u srd, u32 sb) {
  u32 t;
  if constexpr (POL == 1) asm volatile("s_add_u32 m0, %1, %5\n\ts_add_u32 %0, %4, %6\n\tbuffer_load_dwordx4 %2, %3, %0 offen nt lds" : "=&s"(t) : "s"(mb), "v"(voff), "s"(srd), "s"(sb), "i"(MI), "i"(SI) : "memory", "scc");
  else if constexpr (POL == 2) asm volatile("s_add_u32 m0, %1, %5\n\ts_add_u32 %0, %4, %6\n\tbuffer_load_dwordx4 %2, %3, %0 offen sc1 lds" : "=&s"(t) : "s"(mb), "v"(voff), "s"(srd), "s"(sb), "i"(MI), "i"(SI) : "memory", "scc");
  else asm volatile("s_add_u32 m0, %1, %5\n\ts_add_u32 %0, %4, %6\n\tbuffer_load_dwordx4 %2, %3, %0 offen lds" : "=&s"(t) : "s"(mb), "v"(voff), "s"(srd), "s"(sb), "i"(MI), "i"(SI) : "memory", "scc");
}
template <int N> DI void wait_vm() { asm volatile("s_waitcnt vmcnt(%0)" ::"i"(N) : "memory"); SB(); }
template <int N> DI void wait_bar() { asm volatile("s_waitcnt vmcnt(%0) lgkmcnt(0)\n\ts_barrier" ::"i"(N) : "memory"); SB(); }
DI void bar() { asm volatile("s_waitcnt lgkmcnt(0)\n\ts_barrier" ::: "memory"); SB(); }
DI f32x4 mfma(v4u a, v4u b, f32x4 c) {
  return __builtin_amdgcn_mfma_f32_16x16x32_bf16(__builtin_bit_cast(bf16x8, a), __builtin_bit_cast(bf16x8, b), c, 0, 0, 0);
}
DI f32x16 mfma32(v4u a, v4u b, f32x16 c) {
  return __builtin_amdgcn_mfma_f32_32x32x16_bf16(__builtin_bit_cast(bf16x8, a), __builtin_bit_cast(bf16x8, b), c, 0, 0, 0);
}
DI v2u trd(LAS char* p) { return __builtin_bit_cast(v2u, __builtin_amdgcn_ds_read_tr16_b64_v4i16((LAS s16x4*)p)); }
DI v4u trd2(LAS char* p0, LAS char* p1) { v2u a = trd(p0), b = trd(p1); return v4u{a.x, a.y, b.x, b.y}; }
DI v4u lrd(LAS char* p) { return *(LAS v4u*)p; }
DI u32 pk(float a, float b) { return __builtin_bit_cast(u32, __builtin_convertvector(f32x2{a, b}, bf16x2)); }
DI void poll_tags(const GAS u64* p, bool active, u64 tag) {
#pragma unroll 1
  for (u32 spins = 0; spins < SPIN_CAP; ++spins) {
    u64 v = tag;
    if (active) v = __hip_atomic_load(p, RLX_AGENT);
    if (__all(v == tag)) break;
  }
  __builtin_amdgcn_fence(__ATOMIC_ACQUIRE, "wavefront");
}

constexpr float CS = 0.08838834764831845f * 1.4426950408889634f;
constexpr int AU[14] = {14, 15, 16, 17, 18, 19, 10, 11, 12, 13, 9, 6, 7, 8};
constexpr int R0_OFF = 24576, R1_OFF = 0;
constexpr int OP_NONE = -1, OP_POLL = 100;
constexpr int npre(bool late) { return late ? 8 : 4; }
constexpr int NPT = 5;
constexpr int NSEG0 = 14;
constexpr int NGATE = 7;
struct Sched {
  int op[16][NPT];
  int wait[16];
  int chk[16];
};
constexpr Sched make_sched(bool late) {
  Sched t{};
  int seq[160] = {}; int n = 0;
  bool doneA[32] = {}, doneB[24] = {};
  for (int j = 0; j < 3; ++j) doneB[j] = true;
  for (int u = 0; u < npre(late); ++u) { if (late && u == 6) seq[n++] = OP_POLL; seq[n++] = 32 + u; doneA[u] = true; }
  for (int s = 0; s < 16; ++s) {
    if (!late && s == 2) {
      for (int u = npre(late); u < NSEG0; ++u) { seq[n++] = 32 + u; doneA[u] = true; }
      for (int j = 3; j < NGATE; ++j) { seq[n++] = j; doneB[j] = true; }
    }
    const int jb = 3 * (s / 2) + 1 + (s & 1), ua = 2 * s + 1;
    int w = 0, y = 0;
    for (int i = 0; i < n; ++i) {
      const int o = seq[i];
      if (o == OP_POLL) { y = 0; continue; }
      ++y;
      if ((o < 24 && o <= jb) || (o >= 32 && o - 32 <= ua)) w = 0; else ++w;
    }
    t.wait[s] = w;
    t.chk[s] = ((s == 0 && late) || (s == 5 && !late) || s == 7) ? y : -1;
    for (int k = 0; k < NPT; ++k) t.op[s][k] = OP_NONE;
    if (!late && s < 2) continue;
    int k = 0, cnt = 0;
    if ((s == 3 && !late) || s == 5) { t.op[s][k++] = OP_POLL; seq[n++] = OP_POLL; }
    while (cnt < 4) {
      int best = -1, bd = 99;
      for (int j = 0; j < 24; ++j) if (!doneB[j]) {
        const int av = j < 6 ? 0 : 2 * (j / 3 - 2) + 1 + (j % 3 != 0);
        const int dl = 2 * (j / 3) + (j % 3 == 2);
        if (av <= s && dl < bd) { bd = dl; best = j; }
        break;
      }
      for (int u = 0; u < 32; ++u) if (!doneA[u]) {
        const int av = u < 14 ? 0 : (u - 14) / 2 + 1;
        if (av <= s && u / 2 < bd) { bd = u / 2; best = 32 + u; }
        break;
      }
      if (best < 0 || (cnt >= 3 && bd > s + 3)) break;
      if (best < 32) doneB[best] = true; else doneA[best - 32] = true;
      t.op[s][k++] = best; seq[n++] = best; ++cnt;
    }
  }
  return t;
}

template <bool LATE> constexpr Sched SCH = make_sched(LATE);
constexpr bool sched_ok(bool late) {
  const Sched t = make_sched(late);
  int n = 0;
  for (int s = 0; s < 16; ++s)
    for (int k = 0; k < NPT; ++k) {
      const int o = t.op[s][k];
      if (o < 0 || o == OP_POLL) continue;
      ++n;
      if (s >= (o < 32 ? 2 * (o / 3) + (o % 3 == 2) : (o - 32) / 2)) return false;
      if (o < 32 && ((o >= 12 && s < 5) || (o >= 15 && s < 7))) return false;
    }
  return n == (late ? 21 + 32 - npre(true) : 24 - NGATE + 32 - NSEG0);
}
static_assert(sched_ok(false) && sched_ok(true), "load schedule");

DI u64 uniform64(u32 lo, u32 hi) { return ((u64)(u32)__builtin_amdgcn_readfirstlane(hi) << 32) | (u32)__builtin_amdgcn_readfirstlane(lo); }
DI int fresh(int v) { asm volatile("" : "+v"(v)); return v; }
DI void cnt_add(LAS u32* p) { asm volatile("s_waitcnt lgkmcnt(0)\n\ts_mov_b64 exec, 1\n\tds_add_u32 %0, %1\n\ts_mov_b64 exec, -1" ::"v"(p), "v"(1u) : "memory"); SB(); }
DI void cnt_wait(LAS u32* p, u32 n) {
  u32 v, t, spins = SPIN_CAP;
  asm volatile(".Lcw%=:\n\tds_read_b32 %0, %3\n\ts_waitcnt lgkmcnt(0)\n\tv_readfirstlane_b32 %1, %0\n\ts_cmp_ge_u32 %1, %4\n\ts_cbranch_scc1 .Lcx%=\n\t"
               "s_sleep 1\n\ts_sub_u32 %2, %2, 1\n\ts_cbranch_scc0 .Lcw%=\n.Lcx%=:"
               : "=&v"(v), "=&s"(t), "+s"(spins) : "v"(p), "s"(n) : "memory", "scc");
  SB();
}
DI void rbar() { asm volatile("s_barrier" ::: "memory"); SB(); }

struct QLoad {
  v4u srdW, srdX;
  u32 va, vb[3], mu, mx, so, sr;
  DI void next() {
    sfor<0, 4>([&](auto I) {
      dma16<0, I.value * 4096, I.value * 16 * 8192>(mu + so, va, srdW, sr * 64 * 8192);
      if constexpr (I.value < 3) dma16<0, 16384 + I.value * 1024, 0>(mx + so, vb[I.value], srdX, sr * 128);
    });
    so = so == 2 * SLOT ? 0 : so + SLOT;
    sr = (sr + 1) & 15;
  }
  DI void run(int n) {
    next(); next();
#pragma unroll 1
    for (n -= 2; n > 0; --n) { wait_vm<7>(); rbar(); next(); }
  }
};
DI void loader(LAS char* L, QLoad& q, int u, int l, int bid, const char* kc, const char* vc, const char* wo, char* ws, char* wsb) {
  const int xcd = bid & 7, idx = bid >> 3, h = xcd * 4 + (idx & 3), b = idx >> 2;
  const int g = l >> 4, c = l & 15;
  const u32 mu = q.mu;
  const v4u srdK = make_srd(kc + (size_t)(b * 32 + h) * 131072, 131072);
  const v4u srdV = make_srd(vc + (size_t)(b * 32 + h) * 131072, 131072);
  const u32 swz = ((u32)c ^ (2 * (g | ((u & 1) << 2)))) << 4;
  const u32 vk = (4 * u + g) * 256 + (((u32)c ^ (4 * u + g)) << 4), vv = (4 * u + g) * 256 + swz, vwo = (4 * u + g) * 2048 + swz;
  u32 kro = 2 * xcd + (idx >> 1);
  auto kv_at = [&](auto I, auto SL) {
    constexpr int i = I.value;
    constexpr int dst = V_OFF + SL.value * 8192;
    asm volatile("" : "+s"(kro));
    const u32 so = ((i + kro) & 15) * 8192;
    dma16<1, dst, 0>(mu, i < 16 ? vk : vv, i < 16 ? srdK : srdV, so);
    dma16<1, dst + 4096, 4096>(mu, i < 16 ? vk : vv, i < 16 ? srdK : srdV, so);
  };
  auto kv = [&](auto I) { kv_at(I, IC<(I.value + 2) % 10>{}); };
  auto qend = [&](auto X0, auto X1, auto X2) {
    wait_vm<7>(); rbar(); sfor<X0.value, X1.value>(kv);
    wait_vm<2 * (X1.value - X0.value)>(); rbar(); sfor<X1.value, X2.value>(kv);
  };
  qend(IC<0>{}, IC<4>{}, IC<8>{});
  auto rest = [&](auto LT) {
  constexpr bool LATE = decltype(LT)::value;
  const int kq = xcd >> 1, nt = (xcd & 1) * 4 + (idx & 3), pe = b & 1, io = (h & 7) >> 1;
  const v4u srdWo = make_srd(wo + (size_t)kq * 1024 * 2048 + nt * 256, (4096 - kq * 1024) * 2048 - nt * 256);
  if constexpr (LATE) {
    auto k2 = [&](auto I) { kv_at(I, IC<I.value - 6>{}); };
    wait_vm<8>(); rbar(); rbar(); sfor<8, 12>(k2);
    wait_vm<8>(); rbar(); rbar(); sfor<12, 16>(k2);
    wait_vm<8>(); rbar(); rbar(); sfor<20, 22>(kv);
    q.run(16 - SPLIT);
    qend(IC<16>{}, IC<18>{}, IC<20>{});
    rbar();
    sfor<22, 26>(kv);
#pragma unroll
    for (int e = 0; e < 2; ++e)
      asm volatile("s_mov_b32 m0, %0\n\ts_nop 0\n\tbuffer_load_dword %1, %2, %3 offen lds" ::"s"(mu - u * 768 + SCR_OFF), "v"((l >> 1) * 2048 + (l & 1) * 128),
                   "s"(srdWo), "s"(((b >> 1) * 8 + u * 2 + e) * 32 * 2048) : "memory");
  } else {
    sfor<8, 10>(kv);
    LAS u32* cnt = (LAS u32*)(L + BAD_OFF + 4);
    v4u kr[12];
    auto k_reg = [&](auto I) {
      constexpr int i = decltype(I)::value;
      asm volatile("" : "+s"(kro));
      const u32 so = ((i + kro) & 15) * 8192, v0 = vk, v1 = vk + 4096;
      const v4u sd = srdK;
      v4u &d0 = kr[2 * (i - 10)], &d1 = kr[2 * (i - 10) + 1];
      asm volatile("buffer_load_dwordx4 %0, %2, %4, %5 offen nt\n\tbuffer_load_dwordx4 %1, %3, %4, %5 offen nt" : "=&v"(d0), "=&v"(d1) : "v"(v0), "v"(v1), "s"(sd), "s"(so) : "memory");
    };
    LAS char* kw = L + V_OFF + u * 1024 + l * 16;
    auto k_put = [&](auto I, auto N) {
      constexpr int i = decltype(I)::value;
      v4u &d0 = kr[2 * (i - 10)], &d1 = kr[2 * (i - 10) + 1];
      asm volatile("s_waitcnt vmcnt(%2)" : "+v"(d0), "+v"(d1) : "i"(decltype(N)::value) : "memory");
      *(LAS v4u*)(kw + (i - 8) * 8192) = d0;
      *(LAS v4u*)(kw + (i - 8) * 8192 + 4096) = d1;
    };
    sfor<10, 12>(k_reg);
    wait_vm<16>(); rbar();
    sfor<12, 13>(k_reg);
    wait_vm<10>(); cnt_add(cnt);
    sfor<13, 16>(k_reg);
    cnt_wait(cnt + 1, 4);
    k_put(IC<10>{}, IC<10>{}); k_put(IC<11>{}, IC<8>{});
    cnt_add(cnt + 4);
    cnt_wait(cnt + 2, 4); sfor<16, 18>(kv);
    k_put(IC<12>{}, IC<10>{}); k_put(IC<13>{}, IC<8>{}); k_put(IC<14>{}, IC<6>{}); k_put(IC<15>{}, IC<4>{});
    cnt_add(cnt + 3);
    cnt_wait(cnt + 1, 8); sfor<18, 22>(kv);
    cnt_wait(cnt + 2, 8); sfor<22, 26>(kv);
  }
  const v2u tg2 = *(LAS v2u*)(L + BAD_OFF + 24);
  const u64 tag = uniform64(tg2.x, tg2.y);
  const v4u srdO = make_srd(wsb + b * WS_BSZ + B_O + kq * 8 * 24576, 8 * 24576);
  const char* flags = wsb + b * WS_BSZ + B_F + kq * 8 * 128;
  auto head = [&](int d) { return (u32)(2 * ((d + io) & 3) + ((d == 0 || d == 4) ? (h ^ (d >> 2)) & 1 : pe ^ (d >> 2))); };
  auto wo_blk = [&](auto U) {
    constexpr int n = U.value;
    const u32 so = head(n >> 2) * 128 * 2048;
    dma16<0, AU[n % 14] * 8192, (n & 3) * 32 * 2048>(mu, vwo, srdWo, so);
    dma16<0, AU[n % 14] * 8192 + 4096, ((n & 3) * 32 + 16) * 2048>(mu, vwo, srdWo, so);
  };
  const u32 vo = u * 1024 + l * 16;
  auto o_unit = [&](auto J) {
    constexpr int j = J.value, d = j / 3, t = j % 3, dst = ((d & 1) ? R1_OFF : R0_OFF) + t * 8192;
    const u32 so = head(d) * 24576;
    dma16<2, dst, t * 8192>(mu, vo, srdO, so);
    dma16<2, dst + 4096, t * 8192 + 4096>(mu, vo, srdO, so);
  };
  u64 pv;
  auto poll = [&](u32 voff) { if (u == 0) asm volatile("global_load_dwordx2 %0, %1, %2 sc1" : "=v"(pv) : "v"(voff), "s"(flags) : "memory"); };
  auto check = [&](auto N, int voff, bool need) {
    if (u == 0) {
      asm volatile("s_waitcnt vmcnt(%1)" : "+v"(pv) : "i"(N.value) : "memory");
      if (!__all(!need || pv == tag)) poll_tags((const GAS u64*)(flags + voff), need, tag);
    }
  };
  constexpr int T2 = LATE ? 2 : 0, NP = npre(LATE);
  constexpr auto mn = [](int a, int b_) { return a < b_ ? a : b_; };
  wait_vm<LATE ? 8 + T2 : 12>(); rbar();
  rbar(); sfor<26, 28>(kv);
  wait_vm<8 + T2>(); rbar();
  const int row = 24 * u + (l < 24 ? l : 0);
  f32x4 s0 = *(LAS f32x4*)(L + SUM_OFF + row * 32), s1 = *(LAS f32x4*)(L + SUM_OFF + row * 32 + 16);
  sfor<28, 30>(kv);
  rbar();
  asm volatile("" : "+v"(s0), "+v"(s1));
  sfor<30, 32>(kv);
  const float sm = ((s0[0] + s0[1]) + (s0[2] + s0[3])) + ((s1[0] + s1[1]) + (s1[2] + s1[3]));
  ((LAS float*)(L + ST_OFF))[row] = __builtin_amdgcn_rcpf(sm);
  if (!(sm > 0x1p-16f && sm < 0x1p56f)) *(LAS u32*)(L + BAD_OFF) = 1;
  wait_vm<12 + T2>(); bar(); sfor<0, 2>(wo_blk);
  const u32 bad = *(LAS u32*)(L + BAD_OFF);
  wait_vm<12>(); rbar(); sfor<2, 4>(wo_blk);
  wait_vm<12>(); rbar(); sfor<4, mn(6, NP)>(wo_blk);
  wait_vm<2 * mn(6, NP)>(); rbar();
  if constexpr (LATE) poll((l & 7) * 128);
  sfor<6, mn(8, NP)>(wo_blk);
  if (__builtin_expect(__builtin_amdgcn_readfirstlane(bad), 0)) {
    wait_vm<0>(); rbar();
    q.so = 0;
    q.run(16);
    qend(IC<0>{}, IC<0>{}, IC<0>{});
    sfor<0, 4>([&](auto P) {
      sfor<0, 8>([&](auto J) { kv_at(IC<P.value * 8 + J.value>{}, IC<J.value + 2>{}); });
      wait_vm<0>(); rbar(); rbar();
    });
    sfor<0, NP>(wo_blk);
    wait_vm<0>();
  }
  auto publish = [&]() {
    if (u == 1) asm volatile("s_mov_b64 exec, 1\n\tglobal_store_dwordx2 %0, %1, %2 sc1\n\ts_mov_b64 exec, -1" ::"v"((h & 7) * 128), "v"(tag), "s"(flags) : "memory");
  };
  sfor<0, 16>([&](auto S) {
    constexpr int s = S.value;
    if constexpr (!LATE && s == 2) {
      sfor<NP, NSEG0>(wo_blk);
      if (u == 0) poll_tags((const GAS u64*)(flags + l * 128), l < 8 && (l & 1) == pe && l != (h & 7), tag);
      rbar();
      sfor<3, NGATE>(o_unit);
    }
    if constexpr (SCH<LATE>.chk[s] >= 0) {
      if constexpr (s == 0) check(IC<2 * SCH<LATE>.chk[s]>{}, l * 128, l < 8 && (l & 1) == pe);
      if constexpr (s == 5) check(IC<2 * SCH<LATE>.chk[s]>{}, l * 128, l == (int)head(4));
      if constexpr (s == 7) check(IC<2 * SCH<LATE>.chk[s]>{}, l * 128, l < 8 && (l & 1) != pe);
    }
    wait_vm<2 * SCH<LATE>.wait[s]>();
    rbar();
    if constexpr (s == (LATE ? 2 : 0)) publish();
    sfor<0, NPT>([&](auto K) {
      constexpr int o = SCH<LATE>.op[s][K.value];
      if constexpr (o >= 0 && o < 24) o_unit(IC<o>{});
      if constexpr (o >= 32 && o < 64) wo_blk(IC<o - 32>{});
      if constexpr (o == OP_POLL) poll((l & 7) * 128);
    });
  });
  if (!LATE && bid == 0 && u == 3) {
#pragma unroll 1
    for (u32 spins = 0; spins < SPIN_CAP; ++spins) {
      u64 d = 0;
#pragma unroll
      for (int i = 0; i < 4; ++i) d |= __hip_atomic_load((const GAS u64*)(wsb + (2 * i + (l >> 5)) * WS_BSZ + B_F + (l & 31) * 128), RLX_AGENT) ^ tag;
      if (__all(d == 0)) break;
    }
    if (l == 0) __hip_atomic_store((GAS u64*)(ws + WS_EPOCH), tag, RLX_AGENT);
  }
  };
  int early = ~(h ^ b) & 1;
  asm volatile("" : "+s"(early));
  if (early) rest(IC<0>{}); else rest(IC<1>{});
}

template <int S0, int S1, class SY, class RA, class RB>
DI void gemm(f32x4 (&acc)[2][3], SY sync, RA ra, RB rb) {
  v4u fa[2][2], fb[2][3];
  sfor<2 * S0, 2 * S1 + 1>([&](auto HH) {
    constexpr int H = HH.value, s = H >> 1, kk = H & 1;
    if constexpr (kk == 0 && s < S1) sync(IC<s>{});
    sfor<0, 6>([&](auto I) {
      constexpr int i = I.value;
      if constexpr (s < S1) {
        if constexpr (i == 0) fa[kk][0] = ra(IC<s>{}, IC<kk>{}, IC<0>{});
        if constexpr (i >= 1 && i <= 3) fb[kk][i - 1] = rb(IC<s>{}, IC<kk>{}, IC<i - 1>{});
        if constexpr (i == 4) fa[kk][1] = ra(IC<s>{}, IC<kk>{}, IC<1>{});
      }
      if constexpr (H > 2 * S0) {
        constexpr int dt = i / 3, qt = i < 3 ? i : 5 - i;
        if constexpr (H == 1) acc[dt][qt] = mfma(fa[kk ^ 1][dt], fb[kk ^ 1][qt], f32x4{0, 0, 0, 0});
        else acc[dt][qt] = mfma(fa[kk ^ 1][dt], fb[kk ^ 1][qt], acc[dt][qt]);
      }
      SB();
    });
  });
}
template <int S0, int S1, class SY, class RA, class RB>
DI void gemm2(f32x4 (&acc)[2][3], SY sync, RA ra, RB rb) {
  v4u fa[2][2][2], fb[2][2][3];
  sfor<S0, S1 + 1>([&](auto S) {
    constexpr int s = S.value, cur = s & 1, prv = cur ^ 1;
    if constexpr (s < S1) sync(S);
    sfor<0, 12>([&](auto I) {
      constexpr int i = I.value;
      if constexpr (s < S1 && i < 10) {
        constexpr int kk = i / 5, r = i % 5;
        if constexpr (r == 0 || r == 4) fa[cur][kk][r >> 2] = ra(S, IC<kk>{}, IC<(r >> 2)>{});
        else fb[cur][kk][r - 1] = rb(S, IC<kk>{}, IC<r - 1>{});
      }
      if constexpr (s > S0) {
        constexpr int kk = i / 6, dt = (i % 6) / 3, qt = (i % 6) < 3 ? i % 6 : 5 - i % 6;
        if constexpr (s == 1 && kk == 0) acc[dt][qt] = mfma(fa[prv][kk][dt], fb[prv][kk][qt], f32x4{0, 0, 0, 0});
        else acc[dt][qt] = mfma(fa[prv][kk][dt], fb[prv][kk][qt], acc[dt][qt]);
      }
      SB();
    });
  });
}
template <class P, class G>
DI void qproj(LAS char* L, int w, int l, f32x4 (&acc)[2][3], bool late, P pause, G hook) {
  l = fresh(l);
  const int g = l >> 4, c = l & 15, qq = (l >> 2) & 3, p = l & 3, nq = w >> 1, qh = w & 1;
  const u32 fx = 2 * (qq | ((g & 1) << 2));
  LAS char* pa[2][2];
  pa[0][0] = L + (8 * g + qq) * 256 + (((4 * nq + (p >> 1)) ^ fx) << 4) + 8 * (p & 1);
  pa[0][1] = L + (8 * g + qq) * 256 + (((4 * nq + 2 + (p >> 1)) ^ fx) << 4) + 8 * (p & 1);
  pa[1][0] = pa[0][0] + 2 * SLOT; pa[1][1] = pa[0][1] + 2 * SLOT;
  LAS char* pb[2] = {L + 16384 + (48 * qh + c) * 128 + (((u32)g ^ (c >> 1)) << 4), L + 16384 + (48 * qh + c) * 128 + (((u32)(4 + g) ^ (c >> 1)) << 4)};
  asm volatile("" : "+v"(pa[1][0]), "+v"(pa[1][1]), "+v"(pb[0]), "+v"(pb[1]));
  gemm<0, 16>(acc, [&](auto S) {
    if constexpr (S.value == SPLIT) { if (late) pause(); }
    bar();
    if constexpr (S.value == SPLIT) { if (late) hook(); }
  }, [&](auto S, auto KK, auto R) {
    LAS char* q = pa[S.value % 3 == 2][R.value] + (S.value % 3 == 1 ? SLOT : 0) + KK.value * 8192;
    return trd2(q, q + 1024);
  }, [&](auto S, auto KK, auto Q) { return lrd(pb[KK.value] + (S.value % 3) * SLOT + Q.value * 2048); });
}

struct TrPtr { LAS char* p[3][2]; };
DI TrPtr tr_ptrs(LAS char* L, int w, int l) {
  const int g = l >> 4, qq = (l >> 2) & 3, p = l & 3, nq = w >> 1;
  const u32 fx = 2 * (qq | ((g & 1) << 2));
  TrPtr t;
#pragma unroll
  for (int i = 0; i < 2; ++i) {
    t.p[0][i] = L + V_OFF + (4 * g + qq) * 256 + (((4 * nq + 2 * i + (p >> 1)) ^ fx) << 4) + 8 * (p & 1);
    t.p[1][i] = t.p[0][i] + 40960;
    t.p[2][i] = t.p[0][i] - 40960;
    asm volatile("" : "+v"(t.p[0][i]), "+v"(t.p[1][i]), "+v"(t.p[2][i]));
  }
  return t;
}
template <int UN> DI v4u tr_frag(const TrPtr& t, int i) {
  LAS char* q = t.p[UN >= 15 ? 1 : UN >= 10 ? 0 : 2][i] + (UN % 5) * 8192;
  return trd2(q, q + 4096);
}
DI void pack_o(v4u (&of)[3], const f32x4 (&o)[2][3], const float (&r)[3]) {
#pragma unroll
  for (int qt = 0; qt < 3; ++qt)
    of[qt] = v4u{pk(o[0][qt][0] * r[qt], o[0][qt][1] * r[qt]), pk(o[0][qt][2] * r[qt], o[0][qt][3] * r[qt]),
                 pk(o[1][qt][0] * r[qt], o[1][qt][1] * r[qt]), pk(o[1][qt][2] * r[qt], o[1][qt][3] * r[qt])};
}

DI void attn_safe(LAS char* L, int w, int l, v4u (&of)[3], TrPtr& tp) {
  const int nq = w >> 1, qh = w & 1;
  bar();
  f32x4 acc[2][3];
  qproj(L, w, l, acc, false, [&]() {}, [&]() {});
  l = fresh(l);
  LAS float* stm = (LAS float*)(L + ST_OFF) + (l & 15);
  LAS char* pq = L + Q_OFF + l * 16;
  u32 kb_ = V_OFF + (2 + w) * 8192 + (l & 15) * 256 + (((u32)(l >> 4) ^ (l & 15)) << 4);
  {
    LAS char* pqw = L + Q_OFF + (12 * qh + nq) * 1024 + ((l & 15) + 16 * (l >> 5)) * 16 + 8 * ((l >> 4) & 1);
    asm volatile("" : "+v"(stm), "+v"(pq), "+v"(kb_), "+v"(pqw));
    if (w < 6) stm[w * 16] = -__builtin_inff();
#pragma unroll
    for (int i = 0; i < 6; ++i)
      *(LAS v2u*)(pqw + (i >> 1) * 4096 + (i & 1) * 512) = v2u{pk(acc[i & 1][i >> 1][0], acc[i & 1][i >> 1][1]), pk(acc[i & 1][i >> 1][2], acc[i & 1][i >> 1][3])};
  }
  f32x4 S[2][2][6];
  sfor<0, 2>([&](auto PS) {
    constexpr int ps = PS.value;
    bar();
    v4u kf[2][4];
    kb_ = fresh(kb_);
    sfor<0, 8>([&](auto J) { kf[J.value & 1][J.value >> 1] = lrd(L + (kb_ ^ ((J.value >> 1) << 6)) + (J.value & 1) * 4096); });
    SB();
    sfor<0, 6>([&](auto Q) {
      constexpr int QT = Q.value;
      S[ps][0][QT] = S[ps][1][QT] = f32x4{0, 0, 0, 0};
      sfor<0, 4>([&](auto I) {
        const v4u qf = lrd(pq + (QT * 4 + I.value) * 1024);
        S[ps][0][QT] = mfma(kf[0][I.value], qf, S[ps][0][QT]);
        S[ps][1][QT] = mfma(kf[1][I.value], qf, S[ps][1][QT]);
        if constexpr (I.value == 1) SB();
      });
      float m = S[ps][0][QT][0];
      sfor<1, 8>([&](auto I) { m = __builtin_fmaxf(m, S[ps][I.value >> 2][QT][I.value & 3]); });
      __hip_atomic_fetch_max(stm + QT * 16, m, __ATOMIC_RELAXED, __HIP_MEMORY_SCOPE_WORKGROUP);
      SB();
    });
    bar();
  });
  const v4u ones = {0x3f803f80u, 0x3f803f80u, 0x3f803f80u, 0x3f803f80u};
  l = fresh(l);
  LAS float* sts = (LAS float*)(L + SUM_OFF) + (l & 15) * 8;
  asm volatile("" : "+v"(sts));
  v4u pf[2][6];
  sfor<0, 6>([&](auto Q) {
    constexpr int QT = Q.value;
    const float mc = stm[QT * 16] * -CS;
    f32x4 sum = {0, 0, 0, 0};
    sfor<0, 2>([&](auto PS) {
      constexpr int ps = PS.value;
      float e[8];
      sfor<0, 8>([&](auto I) { e[I.value] = __builtin_amdgcn_exp2f(fmaf(S[ps][I.value >> 2][QT][I.value & 3], CS, mc)); });
      pf[ps][QT] = v4u{pk(e[0], e[1]), pk(e[2], e[3]), pk(e[4], e[5]), pk(e[6], e[7])};
      sum = mfma(ones, pf[ps][QT], sum);
    });
    sts[QT * 128 + w] = sum[0];
    SB();
  });
  f32x4 o[2][3];
#pragma unroll
  for (int i = 0; i < 6; ++i) o[i & 1][i >> 1] = f32x4{0, 0, 0, 0};
  l = fresh(l);
  tp = tr_ptrs(L, w, l);
  sfor<0, 2>([&](auto PS) {
    constexpr int ps = PS.value;
    sfor<0, 6>([&](auto Q) { *(LAS v4u*)(L + P_OFF + (w * 6 + Q.value) * 1024 + l * 16) = pf[ps][Q.value]; });
    bar();
    sfor<0, 8>([&](auto KB) {
      constexpr int kb = KB.value;
      const v4u a0 = tr_frag<12 + kb>(tp, 0), a1 = tr_frag<12 + kb>(tp, 1);
      sfor<0, 3>([&](auto Q) {
        const v4u pb = lrd(L + P_OFF + (kb * 6 + 3 * qh + Q.value) * 1024 + l * 16);
        o[0][Q.value] = mfma(a0, pb, o[0][Q.value]);
        o[1][Q.value] = mfma(a1, pb, o[1][Q.value]);
      });
      SB();
    });
    bar();
  });
  float r[3];
#pragma unroll
  for (int qt = 0; qt < 3; ++qt) {
    const f32x4 s0 = *(LAS f32x4*)(sts + (3 * qh + qt) * 128), s1 = *(LAS f32x4*)(sts + (3 * qh + qt) * 128 + 4);
    r[qt] = __builtin_amdgcn_rcpf(((s0[0] + s0[1]) + (s0[2] + s0[3])) + ((s1[0] + s1[1]) + (s1[2] + s1[3])));
  }
  pack_o(of, o, r);
}

DI bool attn_fast(LAS char* L, int w, int l, bool late, v4u k0, u64 tag, v4u (&of)[3], TrPtr& tp) {
  l = fresh(l);
  int g = l >> 4, c = l & 15;
  const int nq = w >> 1, qh = w & 1;
  v4u kf[2][2][4];
  const u32 kr = (l & 7) | ((l & 8) << 1) | ((l & 16) >> 1);
  const u32 kin = V_OFF + w * 8192 + kr * 256 + (((u32)(l >> 5) ^ (kr & 15)) << 4);
  auto readK = [&](auto PS, auto A, auto SL) {
    constexpr int ps = PS.value;
    if (A.value == 2 || (w >> 2) == A.value) {
      const u32 base = fresh(kin) + SL.value * 8192;
      sfor<0, 8>([&](auto S) { kf[ps][S.value >> 2][S.value & 3] = lrd(L + (base ^ (S.value << 5))); SB(); });
    }
    SB();
  };
  LAS char* ref = L + ST_OFF;
  LAS u32* bad = (LAS u32*)(L + BAD_OFF);
  LAS float* sts = (LAS float*)(L + SUM_OFF);
  LAS char* pqw[3];
#pragma unroll
  for (int qt = 0; qt < 3; ++qt) { const int QT = 3 * qh + qt; pqw[qt] = L + Q_OFF + ((QT >> 1) * 8 + 2 * nq) * 1024 + (QT & 1) * 256 + (c + 32 * (g >> 1)) * 16 + 8 * (g & 1); }
  LAS char* prw = ref + (48 * qh + c) * 16 + nq * 4;
  LAS char* pr = ref + (l & 31) * 16;
  LAS char* pq = L + Q_OFF + l * 16;
  LAS char* pw1 = L + P_OFF + w * 6144 + (g & 1) * 1024 + (c + 16 * (g >> 1)) * 16;
  asm volatile("" : "+v"(pqw[0]), "+v"(pqw[1]), "+v"(pqw[2]), "+v"(prw), "+v"(pr), "+v"(pq), "+v"(pw1), "+v"(bad));
  v4u mq = {0xBE80BE80u, 0xBE80BE80u, 0xBE80BE80u, 0xBE80BE80u};
  const u32 one = (c >> 3) == (g & 1) ? 0x3f803f80u : 0u;
  v4u sel = {one, one, one, one};
  asm volatile("" : "+v"(sel), "+v"(mq));
  f32x4 acc[2][3];
  qproj(L, w, l, acc, late, [&]() {
    bar(); readK(IC<0>{}, IC<0>{}, IC<2>{}); bar();
    bar(); readK(IC<0>{}, IC<1>{}, IC<2>{}); bar();
    bar(); readK(IC<1>{}, IC<0>{}, IC<2>{}); bar();
  }, [&]() { readK(IC<1>{}, IC<1>{}, IC<2>{}); });
  {
    SB();
    if (w == 0) bad[l & 7] = (l & 7) == 6 ? (u32)tag : (l & 7) == 7 ? (u32)(tag >> 32) : 0u;
    SB();
    f32x4 part[3];
#pragma unroll
    for (int qt = 0; qt < 3; ++qt) {
      const f32x4 a0 = acc[0][qt] * CS, a1 = acc[1][qt] * CS;
      const v4u qb = v4u{pk(a0[0], a0[1]), pk(a0[2], a0[3]), pk(a1[0], a1[1]), pk(a1[2], a1[3])};
      SB();
      *(LAS v2u*)(pqw[qt]) = v2u{qb.x, qb.y};
      SB();
      *(LAS v2u*)(pqw[qt] + 1024) = v2u{qb.z, qb.w};
      part[qt] = mfma(k0, qb, f32x4{0, 0, 0, 0});
      SB();
    }
#pragma unroll
    for (int qt = 0; qt < 3; ++qt) *(LAS u32*)(prw + qt * 256) = pk(part[qt][0], part[qt][0]);
  }
  bar();
  constexpr int QAH = 4;
  v4u qf[48];
  v4u rf = lrd(pr);
  sfor<0, QAH>([&](auto I) { qf[I.value] = lrd(pq + I.value * 1024); });
  if (!late) { if (w >= 4) cnt_wait(bad + 1, 4); readK(IC<0>{}, IC<2>{}, IC<2>{}); }
  f32x16 S[2];
  const f32x16 z16 = {0, 0, 0, 0, 0, 0, 0, 0, 0, 0, 0, 0, 0, 0, 0, 0};
  f32x4 sum[3];
  v4u p2[6];
  sfor<0, 7>([&](auto GG) {
    constexpr int G = GG.value, ps = G / 3, cur = G & 1, prv = cur ^ 1, G2 = G - 1, Q2 = (G2 + 3) % 3;
    if constexpr (G == 6) tp = tr_ptrs(L, w, fresh(l));
    float e[16];
    u32 pf[8];
    auto unit = [&](auto SP) {
      constexpr int sp = SP.value;
      const v4u pv4 = {pf[4 * sp], pf[4 * sp + 1], pf[4 * sp + 2], pf[4 * sp + 3]};
      if constexpr (G2 < 3) {
        *(LAS v4u*)(pw1 + Q2 * 2048 + sp * 512) = pv4;
        if constexpr (sp == 0) sum[Q2] = mfma(sel, pv4, f32x4{0, 0, 0, 0}); else sum[Q2] = mfma(sel, pv4, sum[Q2]);
        asm volatile("" : "+v"(sum[Q2]));
      } else p2[2 * Q2 + sp] = pv4;
    };
    if constexpr (G < 6) { S[cur] = mfma32(mq, rf, z16); SB(); }
    sfor<0, 8>([&](auto I) {
      constexpr int i = I.value, n = G * 8 + i;
      if constexpr (n == 21) { if (!late) { cnt_wait(bad + 5 - (w >> 2), 4); readK(IC<1>{}, IC<2>{}, IC<0>{}); } }
      if constexpr (n == 3 || n == 26) { if (!late) cnt_add(bad + 2 + (w >> 2)); }
      if constexpr (n + QAH < 48) qf[n + QAH] = lrd(pq + ((n + QAH) % 24) * 1024);
      if constexpr (i == 3 && G < 5) { asm volatile("" : "+v"(pr)); rf = lrd(pr + ((G + 1) % 3) * 512); }
      if constexpr (G < 6) S[cur] = mfma32(kf[ps][i >> 2][i & 3], qf[n < 48 ? n : 0], S[cur]);
      if constexpr (G2 >= 0) {
        if constexpr (i > 0) pf[i - 1] = pk(e[2 * i - 2], e[2 * i - 1]);
        e[2 * i] = __builtin_amdgcn_exp2f(S[prv][2 * i]);
        e[2 * i + 1] = __builtin_amdgcn_exp2f(S[prv][2 * i + 1]);
        if constexpr (i == 5) unit(IC<0>{});
      }
      SB();
    });
    if constexpr (G2 >= 0) { pf[7] = pk(e[14], e[15]); unit(IC<1>{}); SB(); }
  });
  l = fresh(l); g = l >> 4; c = l & 15;
  LAS char* pp1 = L + P_OFF + 3 * qh * 1024 + l * 16;
  asm volatile("" : "+v"(pp1));
  LAS float* psw = sts + (16 * (g >> 1) + c) * 8 + w;
  bar();
  LAS char* pp2 = L + (qh ? P_OFF : Q_OFF) + l * 16;
  LAS char* pw2 = L + w * 3072 + (c + 16 * (g >> 1)) * 16;
  LAS float* psr = (LAS float*)ref + 48 * qh + c;
  f32x4 o[2][3];
  v4u va[2][2], pb[2][3];
  float r[3];
  u32 badv = 0;
  sfor<0, 17>([&](auto KB) {
    constexpr int kb = KB.value, cur = kb & 1, prv = cur ^ 1, sl = (kb + 8) % 10;
    if constexpr (kb >= 2 && kb < 16 && cur == 0) bar();
    sfor<0, 6>([&](auto I) {
      constexpr int i = I.value;
      if constexpr (i < 5 && kb < 16) {
        if constexpr (i == 0 || i == 2) va[cur][i >> 1] = tr_frag<10 + sl>(tp, i >> 1);
        else {
          constexpr int j = i == 1 ? 0 : i - 2;
          if constexpr (kb < 8) pb[cur][j] = lrd(pp1 + (kb * 6 + j) * 1024);
          else pb[cur][j] = lrd(pp2 + ((kb - 8) * 3 + j) * 1024);
        }
      }
      if constexpr (kb > 0) {
        constexpr int dt = i & 1, qt = i >> 1;
        if constexpr (kb == 1) o[dt][qt] = mfma(va[prv][dt], pb[prv][qt], f32x4{0, 0, 0, 0});
        else o[dt][qt] = mfma(va[prv][dt], pb[prv][qt], o[dt][qt]);
      }
      if constexpr (kb == 0) sum[i % 3] = mfma(sel, p2[2 * (i % 3) + i / 3], sum[i % 3]);
      if constexpr (kb == 1 && i >= 3) psw[(i - 3) * 256] = sum[i - 3][0];
      if constexpr (kb >= 2 && kb <= 7 && i == 5) {
        constexpr int q = kb - 2;
        const int QT = (q & ~1) + (g & 1);
        *(LAS v4u*)(pw2 + (QT < 3 ? Q_OFF + QT * 1024 : P_OFF + (QT - 3) * 1024) + (q & 1) * 512) = p2[q];
      }
      if constexpr (kb >= 9 && kb < 12 && i == 5) r[kb - 9] = psr[(kb - 9) * 16];
      if constexpr (kb == 12 && i == 5) badv = *bad;
      SB();
    });
  });
  pack_o(of, o, r);
  return __builtin_amdgcn_readfirstlane(badv) == 0;
}

template <class F>
DI void oproj(LAS char* const (&pb)[3], const TrPtr& tp, bool late, f32x4 (&acc)[2][3], F prep) {
  gemm2<0, 16>(acc, [&](auto S) {
    if constexpr (S.value == 0) { if (!late) wait_vm<0>(); }
    if constexpr (S.value == 2) {
      prep();
      if (!late) bar();
    }
    if constexpr (S.value == 2) wait_bar<0>(); else bar();
  }, [&](auto S, auto KK, auto R) {
    return tr_frag<AU[(2 * S.value + KK.value) % 14]>(tp, R.value);
  }, [&](auto S, auto KK, auto Q) { return lrd(pb[Q.value] + (((S.value >> 1) & 1) ? R1_OFF : R0_OFF) + (S.value & 1) * 12288 + KK.value * 1024); });
}

__global__ void __launch_bounds__(768, 3) k_fused(const char* x, const char* kc, const char* vc, const char* wq, const char* wo, char* out, char* ws, char* wsb) {
  extern __shared__ __attribute__((aligned(16))) char smem[];
  LAS char* L = (LAS char*)smem;
  const int w = __builtin_amdgcn_readfirstlane(threadIdx.x >> 6);
  int l = threadIdx.x & 63;
  const int bid = blockIdx.x, xcd = bid & 7, idx = bid >> 3;
  const int h = xcd * 4 + (idx & 3), b = idx >> 2;
  const bool late = ((h ^ b) & 1) != 0;
  if (__builtin_expect(w >= 8, 1)) {
    const int u = w - 8, g = l >> 4, c = l & 15;
    QLoad q;
    q.va = (4 * u + g) * 8192 + (((u32)c ^ (2 * (g | (((u >> 1) & 1) << 2)))) << 4);
#pragma unroll
    for (int i = 0; i < 3; ++i) { const u32 r = 24 * u + 8 * i + (l >> 3); q.vb[i] = r * 2048 + ((((u32)l & 7) ^ ((r >> 1) & 7)) << 4); }
    q.mu = (u32)(size_t)L + u * 1024; q.mx = (u32)(size_t)L + u * 3072;
    q.so = 0; q.sr = (2 * b + 4 * (h & 3) + (xcd & 1)) & 15;
    asm volatile("" : "+v"(q.va), "+v"(q.vb[0]), "+v"(q.vb[1]), "+v"(q.vb[2]), "+s"(q.mu), "+s"(q.mx), "+s"(q.sr));
    SB();
    q.srdW = make_srd(wq + h * 256, 1024 * 8192 - h * 256);
    q.srdX = make_srd(x + (size_t)b * 96 * 2048, 96 * 2048);
    q.run(late ? SPLIT : 16);
    loader(L, q, u, l, bid, kc, vc, wo, ws, wsb);
    return;
  }
  const char* krow0 = kc + (size_t)(b * 32 + h) * 131072 + 64 * (w >> 1) + 8 * (l >> 4);
  const v2u k0a = *(const GAS v2u*)krow0, k0b = *(const GAS v2u*)(krow0 + 32);
  u64 tag = __hip_atomic_load((const GAS u64*)(ws + WS_EPOCH), RLX_AGENT) * 6364136223846793005ull + 1442695040888963407ull;
  tag = uniform64((u32)tag, (u32)(tag >> 32));
  v4u k0 = {k0a.x, k0a.y, k0b.x, k0b.y};
  asm volatile("" : "+v"(k0));
  const __amdgpu_buffer_rsrc_t rsP = __builtin_amdgcn_make_buffer_rsrc(wsb + b * WS_BSZ, 0, WS_BSZ, 0x20000);
  const int nq = w >> 1, qh = w & 1;
  const int kq = xcd >> 1;
  u32 opiece = ((nq >> 1) * 12 + 6 * qh + (nq & 1)) * 1024, osoff = B_O + h * 24576 + opiece;
  const int keep = (kq - nq) & 3;
  int qj[3] = {(0x01 >> (2 * keep)) & 3, (0x5a >> (2 * keep)) & 3, (0xa4 >> (2 * keep)) & 3};
  asm volatile("" : "+s"(opiece), "+s"(osoff), "+s"(qj[0]), "+s"(qj[1]), "+s"(qj[2]));
  v4u of[3];
  TrPtr tp;
  if (__builtin_expect(!attn_fast(L, w, l, late, k0, tag, of, tp), 0)) attn_safe(L, w, l, of, tp);
  l = fresh(l);
#pragma unroll
  for (int qt = 0; qt < 3; ++qt) __builtin_amdgcn_raw_buffer_store_b128(of[qt], rsP, l * 16 + (qt & 1) * 2048, osoff + (qt >> 1) * 4096, 16);
  SB();
#pragma unroll
  for (int qt = 0; qt < 3; ++qt) *(LAS v4u*)(L + R0_OFF + opiece + qt * 2048 + l * 16) = of[qt];
  u32 sto[3], ldo[3], oo, vl, vout;
  GAS u64 *fo, *fi;
  f32x4 acc[2][3];
  LAS char* const pb[3] = {L + (3 * qh + qj[0]) * 2048 + l * 16, L + (3 * qh + qj[1]) * 2048 + l * 16, L + (3 * qh + qj[2]) * 2048 + l * 16};
  oproj(pb, tp, late, acc, [&]() {
    int me = bid & 31;
    asm volatile("" : "+s"(me), "+v"(l));
    oo = (b * 96 + 48 * qh + 16 * keep) * 2048 + (((me & 1) * 4 + (me >> 3)) * 128 + nq * 32) * 2;
#pragma unroll
    for (int j = 0; j < 3; ++j) {
      sto[j] = B_P + ((((me & 25) | (((nq + qj[j]) & 3) << 1)) * 8 + w) * 4 + kq) * 1024;
      ldo[j] = B_P + ((me * 8 + w) * 4 + ((kq + 1 + j) & 3)) * 1024;
    }
    GAS u64* fl = (GAS u64*)(wsb + b * WS_BSZ + B_X);
    fo = fl + ((((me & 25) | (((nq + l) & 3) << 1)) * 8 + w) * 4 + kq) * 16;
    fi = fl + ((me * 8 + w) * 4 + (l & 3)) * 16;
    vl = l * 16; vout = (l & 15) * 2048 + ((l >> 4) & 1) * 32 + (l >> 5) * 16;
    asm volatile("" : "+s"(sto[0]), "+s"(sto[1]), "+s"(sto[2]), "+s"(ldo[0]), "+s"(ldo[1]), "+s"(ldo[2]), "+s"(oo), "+v"(fo), "+v"(fi), "+v"(vl), "+v"(vout));
  });
  v4u snd[3];
#pragma unroll
  for (int j = 0; j < 3; ++j) {
    snd[j] = v4u{pk(acc[0][j][0], acc[0][j][1]), pk(acc[0][j][2], acc[0][j][3]), pk(acc[1][j][0], acc[1][j][1]), pk(acc[1][j][2], acc[1][j][3])};
    if (j < 2 || keep == 3) __builtin_amdgcn_raw_buffer_store_b128(snd[j], rsP, vl, sto[j], 16);
  }
  asm volatile("" ::"v"(snd[0]), "v"(snd[1]), "v"(snd[2]));
  wait_vm<0>();
  if (l < 3 && l != keep) __hip_atomic_store(fo, tag, RLX_AGENT);
  if (keep < 3) {
    poll_tags(fi, l < 4 && l != kq, tag);
    v4u part[3];
#pragma unroll
    for (int j = 0; j < 3; ++j) part[j] = __builtin_amdgcn_raw_buffer_load_b128(rsP, vl, ldo[j], 16);
    u32 r[4];
#pragma unroll
    for (int i = 0; i < 4; ++i) {
      float s[2];
#pragma unroll
      for (int e = 0; e < 2; ++e) {
        float t[3];
#pragma unroll
        for (int j = 0; j < 3; ++j) t[j] = __uint_as_float(e ? (part[j][i] & 0xffff0000u) : (part[j][i] << 16));
        s[e] = (acc[i >> 1][2][2 * (i & 1) + e] + t[0]) + (t[1] + t[2]);
      }
      r[i] = pk(s[0], s[1]);
    }
    const auto x = __builtin_amdgcn_permlane16_swap(r[0], r[2], false, false), y = __builtin_amdgcn_permlane16_swap(r[1], r[3], false, false);
    __builtin_amdgcn_raw_buffer_store_b128(v4u{x[0], y[0], x[1], y[1]}, __builtin_amdgcn_make_buffer_rsrc(out, 0, 768 * 2048, 0x20000), vout, oo, 0);
  }
}

extern "C" void kernel_launch(void* const* d_in, const int* in_sizes, int n_in, void* d_out, int out_size, void* d_ws, size_t ws_size, hipStream_t stream) {
  (void)hipFuncSetAttribute((const void*)k_fused, hipFuncAttributeMaxDynamicSharedMemorySize, LDS_BYTES);
  k_fused<<<256, 768, LDS_BYTES, stream>>>((const char*)d_in[0], (const char*)d_in[1], (const char*)d_in[2], (const char*)d_in[3], (const char*)d_in[4], (char*)d_out, (char*)d_ws, (char*)d_ws + WS_B);
}
```
